```python
import torch

def _softmax(x, axis=-1):
    return torch.softmax(x, dim=axis)
def _gelu(x):
    return torch.nn.functional.gelu(x, approximate="tanh")
def _silu(x):
    return torch.nn.functional.silu(x)
def _sigmoid(x):
    return torch.sigmoid(x)
def _layernorm(x, axis=-1, eps=1e-5):
    m = x.mean(dim=axis, keepdim=True)
    v = x.var(dim=axis, keepdim=True, unbiased=False)
    return (x - m) / torch.sqrt(v + eps)
def _scatter_add(tgt, idx, src, axis=0):
    return tgt.clone().index_add_(axis, idx, src)
def _segment_sum(src, seg, n):
    out = torch.zeros((n,) + src.shape[1:], dtype=src.dtype)
    return out.index_add_(0, seg, src)
def _pad(x, pw):
    flat = tuple(v for pair in reversed(pw) for v in pair)
    return torch.nn.functional.pad(x, flat)

kernel_name = 'gated_mix__sliding_local_dilated__linear_attn_block__prompt'

def setup_inputs():

    _w = (lambda a: (lambda t: t.to(torch.bfloat16) if t.is_floating_point() else t)(torch.as_tensor(a)))
    def _A_setup():
        B, S, D = 1, 2048, 448
        return {
            "x": _w((lambda *s: torch.randn(*s, dtype=torch.bfloat16) * 0.05)(B, S, D)),
            "wqkv": _w((lambda *s: torch.randn(*s, dtype=torch.bfloat16) * 0.8)(D, 3 * D)),
            "wo": _w((lambda *s: torch.randn(*s, dtype=torch.bfloat16) * 0.05)(D, D)),
        }
    _A_in = _A_setup()

    def _B_setup():
        B, S, D = 1, 2048, 448
        return {
            "x": _w((lambda *s: torch.randn(*s, dtype=torch.bfloat16) * 0.05)(B, S, D)),
            "wqkv": _w((lambda *s: torch.randn(*s, dtype=torch.bfloat16) * 1.6)(D, 3 * D)),
            "wo": _w((lambda *s: torch.randn(*s, dtype=torch.bfloat16) * 0.4)(D, D)),
        }
    _B_in = _B_setup()

    return {
        "A_x": _A_in["x"],
        "A_wqkv": _A_in["wqkv"],
        "A_wo": _A_in["wo"],
        "B_x": _B_in["x"],
        "B_wqkv": _B_in["wqkv"],
        "B_wo": _B_in["wo"],
        "gate_w": _w((lambda *s: torch.randn(*s, dtype=torch.bfloat16) * 0.05)(448) * 16.0),
    }

def reference(A_x, A_wqkv, A_wo, B_x, B_wqkv, B_wo, gate_w):
    def _A_ref(x, wqkv, wo):
        B, S, D = x.shape
        H = 16; Dh = D // H
        W, dil = 8, 2
        qkv = (lambda x, *d: x.permute(*d))((x @ wqkv).reshape(B, S, 3, H, Dh), 2, 0, 3, 1, 4)
        q, k, v = qkv[0], qkv[1], qkv[2]
        scale = Dh ** -0.5
        pos = torch.arange(S)
        off = torch.arange(W) * dil
        idx = pos[:, None] - off[None, :]
        mask = idx >= 0
        idx_c = torch.where(mask, idx, 0).reshape(-1)
        kg = k[:, :, idx_c, :].reshape(B, H, S, W, Dh)
        vg = v[:, :, idx_c, :].reshape(B, H, S, W, Dh)
        logits = torch.einsum("bhsd,bhswd->bhsw", q, kg) * scale
        logits = torch.where(mask[None, None, :, :], logits, -1e9)
        attn = _softmax(logits, axis=-1)
        o = torch.einsum("bhsw,bhswd->bhsd", attn, vg)
        return (lambda x, *d: x.permute(*d))(o, 0, 2, 1, 3).reshape(B, S, D) @ wo

    def _B_ref(x, wqkv, wo):
        B, S, D = x.shape
        H = 16; Dh = D // H
        qkv = (lambda x, *d: x.permute(*d))((x @ wqkv).reshape(B, S, 3, H, Dh), 2, 0, 3, 1, 4)
        q, k, v = qkv[0], qkv[1], qkv[2]
        phi = lambda t: torch.where(t > 0, t + 1, torch.exp(t)) + 1e-6
        pq, pk = phi(q), phi(k)
        kv = torch.einsum("bhsd,bhse->bhde", pk, v)
        z = torch.einsum("bhsd->bhd", pk)
        num = torch.einsum("bhqd,bhde->bhqe", pq, kv)
        den = torch.einsum("bhqd,bhd->bhq", pq, z)[..., None] + 1e-6
        o = num / den
        return (lambda x, *d: x.permute(*d))(o, 0, 2, 1, 3).reshape(B, S, D) @ wo

    x = A_x
    g = _sigmoid((x * gate_w).sum(-1, keepdims=True))
    a = _A_ref(x, A_wqkv, A_wo)
    b = _B_ref(x, B_wqkv, B_wo)
    return g * a + (1 - g) * b
```

```cpp
#include <hip/hip_runtime.h>
#include <hip/hip_bf16.h>

typedef __bf16 bf16x8 __attribute__((ext_vector_type(8)));
typedef __bf16 bf16x4 __attribute__((ext_vector_type(4)));
typedef __bf16 bf16x2 __attribute__((ext_vector_type(2)));
typedef float f32x4 __attribute__((ext_vector_type(4)));
typedef float f32x8 __attribute__((ext_vector_type(8)));
typedef float f32x16 __attribute__((ext_vector_type(16)));
typedef float f32x2 __attribute__((ext_vector_type(2)));
typedef unsigned u32x4 __attribute__((ext_vector_type(4)));
typedef unsigned long long u64;
#define LAS __attribute__((address_space(3)))
#define GAS __attribute__((address_space(1)))
#define DI __device__ __forceinline__
typedef LAS char* lptr;

constexpr int LDS_HALO = 0;
constexpr int LDS_SLOT = 15360;
constexpr int SLOT_SZ  = 40960;
constexpr int LDS_AX   = 45056;
constexpr int LDS_XR   = 49152;
constexpr int LDS_KVF  = 41984;
constexpr int LDS_WO2  = 32768;
constexpr int LDS_CNT  = 40960;
constexpr int LDS_WO   = 56320;
constexpr int LDS_TOTAL = 163840;

constexpr int WS_EPOCH = 0;
constexpr int WS_FLAGB = 8192;
constexpr int WS_FLAGA = 16384;
constexpr int WS_GATE  = 24576;
constexpr int WS_FLAGK = 32768;
constexpr int WS_KVP   = 1 << 20;
constexpr int WS_OA    = 2 << 20;
constexpr int WS_OB    = 4 << 20;
constexpr int WS_BYTES = 6 << 20;

constexpr unsigned CODE_LINES = 164;
#define MFMA(a, b, c) __builtin_amdgcn_mfma_f32_16x16x32_bf16((a), (b), (c), 0, 0, 0)
#define MFMA32(a, b, c) __builtin_amdgcn_mfma_f32_32x32x16_bf16((a), (b), (c), 0, 0, 0)
#define WAITV(n) asm volatile("s_waitcnt vmcnt(" #n ")" ::: "memory")
#define LGKM0() __builtin_amdgcn_s_waitcnt(0xC07F)
#define RBAR() asm volatile("s_barrier" ::: "memory")
#define BAR() do { LGKM0(); RBAR(); } while (0)
#define WAIT_BAR(N) asm volatile("s_waitcnt vmcnt(" #N ")\n\ts_barrier" ::: "memory")
#define MEMF() asm volatile("" ::: "memory")
#define SBAR() __builtin_amdgcn_sched_barrier(0)
#define PIN(x) asm volatile("" : "+v"(x))
#define MAXF(a, b) __builtin_elementwise_maximum((a), (b))
#define SGB(mask, n) __builtin_amdgcn_sched_group_barrier(mask, n, 0)
#define LD16(off) __builtin_amdgcn_raw_buffer_load_b128(rs, (off), 0, 16)
#define ST16(v, off) __builtin_amdgcn_raw_buffer_store_b128((v), rs, (off), 0, 16)

DI void glds16(const char* sbase, unsigned voff, unsigned lds) {
  asm volatile("s_mov_b32 m0, %2\n\ts_nop 0\n\tglobal_load_lds_dwordx4 %0, %1" :: "v"(voff), "s"(sbase), "s"(lds) : "memory");
}
DI void glds_chunk(const char* xb, unsigned x0, unsigned x1, unsigned x2, unsigned x3, const char* wb, unsigned w0, unsigned w1, unsigned lds) {
  asm volatile("s_mov_b32 m0, %8\n\ts_nop 0\n\t"
               "global_load_lds_dwordx4 %0, %6\n\ts_add_u32 m0, m0, 4096\n\ts_nop 0\n\t"
               "global_load_lds_dwordx4 %1, %6\n\ts_add_u32 m0, m0, 4096\n\ts_nop 0\n\t"
               "global_load_lds_dwordx4 %2, %6\n\ts_add_u32 m0, m0, 4096\n\ts_nop 0\n\t"
               "global_load_lds_dwordx4 %3, %6\n\ts_add_u32 m0, m0, 4096\n\ts_nop 0\n\t"
               "global_load_lds_dwordx4 %4, %7\n\ts_add_u32 m0, m0, 7296\n\ts_nop 0\n\t"
               "global_load_lds_dwordx4 %4, %7 offset:896\n\ts_add_u32 m0, m0, 7296\n\ts_nop 0\n\t"
               "global_load_lds_dwordx4 %4, %7 offset:1792\n\ts_sub_u32 m0, m0, 10496\n\ts_nop 0\n\t"
               "global_load_lds_dwordx4 %5, %7\n\ts_add_u32 m0, m0, 7296\n\ts_nop 0\n\t"
               "global_load_lds_dwordx4 %5, %7 offset:896\n\ts_add_u32 m0, m0, 7296\n\ts_nop 0\n\t"
               "global_load_lds_dwordx4 %5, %7 offset:1792"
               :: "v"(x0), "v"(x1), "v"(x2), "v"(x3), "v"(w0), "v"(w1), "s"(xb), "s"(wb), "s"(lds) : "memory", "scc");
}
DI void glds_chunk_b(const char* xb, unsigned x0, unsigned x1, unsigned x2, unsigned x3, const char* wb, unsigned w0, unsigned w1, unsigned ldsx, unsigned ldsw) {
  asm volatile("s_mov_b32 m0, %8\n\ts_nop 0\n\t"
               "global_load_lds_dwordx4 %0, %6\n\ts_add_u32 m0, m0, 4096\n\ts_nop 0\n\t"
               "global_load_lds_dwordx4 %1, %6\n\ts_add_u32 m0, m0, 4096\n\ts_nop 0\n\t"
               "global_load_lds_dwordx4 %2, %6\n\ts_add_u32 m0, m0, 4096\n\ts_nop 0\n\t"
               "global_load_lds_dwordx4 %3, %6\n\ts_mov_b32 m0, %9\n\ts_nop 0\n\t"
               "global_load_lds_dwordx4 %4, %7\n\ts_add_u32 m0, m0, 7296\n\ts_nop 0\n\t"
               "global_load_lds_dwordx4 %4, %7 offset:896\n\ts_sub_u32 m0, m0, 3200\n\ts_nop 0\n\t"
               "global_load_lds_dwordx4 %5, %7\n\ts_add_u32 m0, m0, 7296\n\ts_nop 0\n\t"
               "global_load_lds_dwordx4 %5, %7 offset:896"
               :: "v"(x0), "v"(x1), "v"(x2), "v"(x3), "v"(w0), "v"(w1), "s"(xb), "s"(wb), "s"(ldsx), "s"(ldsw) : "memory", "scc");
}
template <int N>
DI void glds_wq(const char* base, unsigned voff, unsigned lds) {
  asm volatile("s_mov_b32 m0, %2\n\ts_nop 0\n\t"
               ".rept %c3\n\tglobal_load_lds_dwordx4 %0, %1\n\ts_add_u32 m0, m0, 4096\n\tv_add_u32 %0, 0x15000, %0\n\t.endr"
               : "+v"(voff) : "s"(base), "s"(lds), "i"(N) : "memory", "scc");
}
template <int N>
DI void glds_wo(const char* base, unsigned voff, unsigned lds) {
  asm volatile("s_mov_b32 m0, %2\n\ts_nop 0\n\t"
               ".rept %c3\n\tglobal_load_lds_dwordx4 %0, %1\n\ts_add_u32 m0, m0, 4096\n\tv_add_u32 %0, 0x7000, %0\n\t.endr"
               : "+v"(voff) : "s"(base), "s"(lds), "i"(N) : "memory", "scc");
}
DI bf16x8 lds_b128(lptr smem, int off) { return *(const LAS bf16x8*)(smem + off); }
DI bf16x4 lds_tr(lptr smem, int off) { return __builtin_amdgcn_ds_read_tr16_b64_v4bf16((LAS bf16x4*)(smem + off)); }
DI bf16x8 cat(bf16x4 a, bf16x4 b) { return __builtin_shufflevector(a, b, 0, 1, 2, 3, 4, 5, 6, 7); }
DI bf16x4 pk4(f32x4 v) { return __builtin_convertvector(v, bf16x4); }
DI bf16x8 asbf(u32x4 v) { return __builtin_bit_cast(bf16x8, v); }
DI u32x4 asu(bf16x8 v) { return __builtin_bit_cast(u32x4, v); }
DI lptr mklp(unsigned a) { PIN(a); return (lptr)(size_t)a; }
DI f32x4 phi4(f32x4 u) {
  f32x4 r;
#pragma unroll
  for (int j = 0; j < 4; ++j)
    r[j] = MAXF(u[j], __builtin_amdgcn_fmed3f(__builtin_amdgcn_exp2f(__builtin_fmaf(u[j], 1.4426950408889634f, -1.4426950408889634f)), 0.f, 1.f)) + 1e-6f;
  return r;
}
DI float rbf(float x) { return (float)(__bf16)x; }
DI float xl16(float x, bool mx) {
  auto a = __builtin_amdgcn_permlane16_swap(__float_as_uint(x), __float_as_uint(x), false, false);
  return mx ? MAXF(__uint_as_float(a[0]), __uint_as_float(a[1])) : __uint_as_float(a[0]) + __uint_as_float(a[1]);
}
DI float xl32(float x, bool mx) {
  auto a = __builtin_amdgcn_permlane32_swap(__float_as_uint(x), __float_as_uint(x), false, false);
  return mx ? MAXF(__uint_as_float(a[0]), __uint_as_float(a[1])) : __uint_as_float(a[0]) + __uint_as_float(a[1]);
}
DI u64 mix64(u64 z) { z += 0x9E3779B97F4A7C15ull; z = (z ^ (z >> 30)) * 0xBF58476D1CE4E5B9ull; z = (z ^ (z >> 27)) * 0x94D049BB133111EBull; return z ^ (z >> 31); }
DI u64 ald(const char* p) { return __hip_atomic_load((GAS u64*)p, __ATOMIC_RELAXED, __HIP_MEMORY_SCOPE_AGENT); }
DI void ast(char* p, u64 v) { __hip_atomic_store((GAS u64*)p, v, __ATOMIC_RELAXED, __HIP_MEMORY_SCOPE_AGENT); }
DI u64 read_tag(const char* ws) {
  u64 t = mix64(*(const __attribute__((address_space(4))) u64*)(ws + WS_EPOCH));
  asm volatile("" : "+s"(t) :: "memory");
  return t;
}
constexpr unsigned SPIN_CAP = 1u << 19;

struct P1 { f32x4 acc[4][6]; f32x4 accx[2]; };
struct Frag { bf16x8 x[4], xe, w[6]; };

template <int MODE, int KS>
DI void load_frag(Frag& f, lptr ax, lptr aex, lptr aw0, lptr aw1) {
  constexpr int NW = MODE == 0 ? 6 : 4;
  f.x[0] = lds_b128(ax, 0);
  f.w[0] = cat(lds_tr(aw0, KS * 4096), lds_tr(aw0, KS * 4096 + 512));
  f.x[1] = lds_b128(ax, 2048);
  f.x[2] = lds_b128(ax, 4096);
  f.x[3] = lds_b128(ax, 6144);
#pragma unroll
  for (int j = 1; j < NW; ++j) {
    lptr a = (j & 1) ? aw1 : aw0;
    f.w[j] = cat(lds_tr(a, (j >> 1) * 8192 + KS * 4096), lds_tr(a, (j >> 1) * 8192 + KS * 4096 + 512));
  }
  if (MODE == 0) f.xe = lds_b128(aex, 0);
}
template <int MODE, int SEL>
DI void mma_frag(P1& st, const Frag& f) {
  constexpr int NW = MODE == 0 ? 6 : 4;
#pragma unroll
  for (int j = 0; j < NW; ++j) {
    const bool T = MODE == 0 && j < 4;
#pragma unroll
    for (int i = 0; i < 4; ++i) st.acc[i][j] = T ? MFMA(f.w[j], f.x[i], st.acc[i][j]) : MFMA(f.x[i], f.w[j], st.acc[i][j]);
  }
  if (MODE == 0) {
#pragma unroll
    for (int t = 0; t < 2; ++t) st.accx[t] = MFMA(f.w[2 + 2 * SEL + t], f.xe, st.accx[t]);
  }
}
template <int MODE>
DI void interleave() {
  constexpr int NR = MODE == 0 ? 17 : 12, NM = MODE == 0 ? 26 : 16;
#pragma unroll
  for (int j = 0; j < NR; ++j) { SGB(0x8, 1); SGB(0x100, 1); }
  SGB(0x8, NM - NR);
}

template <int MODE, int SEL>
DI void k_pass(P1& st, lptr smem, int lane, int w) {
  constexpr int NW = MODE == 0 ? 6 : 4;
  constexpr int XB = MODE == 0 ? LDS_SLOT : LDS_XR, XS = MODE == 0 ? SLOT_SZ : 16384;
  constexpr int WB = MODE == 0 ? LDS_SLOT + 16384 : 0, WSZ = MODE == 0 ? SLOT_SZ : 16384;
  const int mh = w & 1, hw = w >> 1;
  const int g = lane >> 4, l15 = lane & 15, q = l15 >> 2, pp = lane & 3;
  const unsigned b = (unsigned)(size_t)smem;
  const unsigned xo0 = b + XB + (mh * 64 + l15) * 128 + ((g ^ (l15 >> 1)) << 4);
  const int fk = ((q >> 1) & 1) | ((g & 1) << 1);
  unsigned wo_[2];
#pragma unroll
  for (int t2 = 0; t2 < 2; ++t2) { const int Q = 7 * hw + 4 * t2 + pp; wo_[t2] = b + WB + (8 * g + q) * 128 + (((Q >> 1) ^ (fk << 1)) << 4) + (Q & 1) * 8; }
  const unsigned exX = b + LDS_HALO + l15 * 128 + ((g ^ (l15 >> 1)) << 4);
#pragma unroll
  for (int i = 0; i < 4; ++i)
#pragma unroll
    for (int j = 0; j < NW; ++j) { const float c0 = (MODE != 0 && j < 2) ? 1.f : 0.f; st.acc[i][j] = f32x4{c0, c0, c0, c0}; PIN(st.acc[i][j]); }
  if (MODE == 0) { st.accx[0] = st.accx[1] = f32x4{0.f, 0.f, 0.f, 0.f}; PIN(st.accx[0]); PIN(st.accx[1]); }
  Frag F0, F1;
  int sw = 0, c = 0;
  lptr ax, aex, ax1, aex1, aw0, aw1;
  auto addr = [&]() {
    const unsigned a0 = xo0 + (MODE == 0 ? sw : c * XS), a1 = exX + c * 2048;
    ax = mklp(a0); aw0 = mklp(wo_[0] + sw); aw1 = mklp(wo_[1] + sw); ax1 = mklp(a0 ^ 64u);
    if (MODE == 0) { aex = mklp(a1); aex1 = mklp(a1 ^ 64u); } else { aex = ax; aex1 = ax; }
  };
  addr();
  BAR();
  load_frag<MODE, 0>(F0, ax, aex, aw0, aw1);
  LGKM0();
  SBAR();
#pragma unroll 1
  for (int i = 0; i < 6; ++i) {
    load_frag<MODE, 1>(F1, ax1, aex1, aw0, aw1);
    mma_frag<MODE, SEL>(st, F0);
    interleave<MODE>();
    SBAR();
    sw = sw == 2 * WSZ ? 0 : sw + WSZ; ++c;
    addr();
    LGKM0();
    if (i == 1) WAITV(0);
    RBAR();
    SBAR();
    load_frag<MODE, 0>(F0, ax, aex, aw0, aw1);
    mma_frag<MODE, SEL>(st, F1);
    interleave<MODE>();
    SBAR();
    LGKM0();
    SBAR();
  }
  load_frag<MODE, 1>(F1, ax1, aex1, aw0, aw1);
  mma_frag<MODE, SEL>(st, F0);
  interleave<MODE>();
  SBAR();
  LGKM0();
  RBAR();
  SBAR();
  mma_frag<MODE, SEL>(st, F1);
}

struct FragQ { bf16x8 x[8], w[4]; };
DI void load_q(FragQ& f, lptr ax, lptr ax1, lptr aw0, lptr aw1) {
#pragma unroll
  for (int ks = 0; ks < 2; ++ks) {
    f.w[2 * ks] = cat(lds_tr(aw0, ks * 4096), lds_tr(aw0, ks * 4096 + 512));
    f.w[2 * ks + 1] = cat(lds_tr(aw1, ks * 4096), lds_tr(aw1, ks * 4096 + 512));
#pragma unroll
    for (int i = 0; i < 4; ++i) f.x[4 * ks + i] = lds_b128(ks ? ax1 : ax, i * 2048);
  }
}
DI void mma_q(P1& st, const FragQ& f) {
#pragma unroll
  for (int ks = 0; ks < 2; ++ks)
#pragma unroll
    for (int i = 0; i < 4; ++i)
#pragma unroll
      for (int j = 0; j < 2; ++j) st.acc[i][j] = MFMA(f.w[2 * ks + j], f.x[4 * ks + i], st.acc[i][j]);
}
DI void q_pass(P1& st, lptr smem, int lane, int w) {
  const int mh = w & 1, hw = w >> 1;
  const int g = lane >> 4, l15 = lane & 15, q = l15 >> 2, pp = lane & 3;
  const unsigned b = (unsigned)(size_t)smem;
  const unsigned xo0 = b + LDS_XR + (mh * 64 + l15) * 128 + ((g ^ (l15 >> 1)) << 4);
  const int fk = ((q >> 1) & 1) | ((g & 1) << 1);
  unsigned wo_[2];
#pragma unroll
  for (int t2 = 0; t2 < 2; ++t2) { const int Q = 7 * hw + 4 * t2 + pp; wo_[t2] = b + (8 * g + q) * 128 + (((Q >> 1) ^ (fk << 1)) << 4) + (Q & 1) * 8; }
#pragma unroll
  for (int i = 0; i < 4; ++i) st.acc[i][0] = st.acc[i][1] = f32x4{1.f, 1.f, 1.f, 1.f};
  FragQ G0, G1;
  int sw = 16384, c = 0;
  lptr ax, ax1, aw0, aw1;
  auto next = [&]() {
    const unsigned a0 = xo0 + c * 16384;
    ax = mklp(a0); ax1 = mklp(a0 ^ 64u); aw0 = mklp(wo_[0] + sw); aw1 = mklp(wo_[1] + sw);
    sw = sw == 5 * 8192 ? 0 : sw + 8192; ++c;
  };
  auto il = [&]() {
#pragma unroll
    for (int j = 0; j < 8; ++j) { SGB(0x8, 1); SGB(0x100, 2); }
    SGB(0x8, 8);
  };
  next();
  load_q(G0, ax, ax1, aw0, aw1);
  next();
  BAR();
  SBAR();
#pragma unroll 1
  for (int i = 0; i < 3; ++i) {
    load_q(G1, ax, ax1, aw0, aw1);
    mma_q(st, G0);
    il();
    SBAR();
    next();
    LGKM0();
    if (i == 1) RBAR();
    SBAR();
    load_q(G0, ax, ax1, aw0, aw1);
    mma_q(st, G1);
    il();
    SBAR();
    next();
    LGKM0();
    SBAR();
  }
  mma_q(st, G0);
}

__global__ __launch_bounds__(512, 2) void gated_mix__sliding_local_dilated__linear_attn_block__prompt_kernel(const char* __restrict__ x, const char* __restrict__ wA, const char* __restrict__ woA,
                                                        const char* __restrict__ wB, const char* __restrict__ woB, const char* __restrict__ gw,
                                                        char* __restrict__ out, char* ws) {
  extern __shared__ __attribute__((aligned(16))) char smem_[];
  lptr smem = (lptr)smem_;
  const unsigned lds0 = (unsigned)(size_t)smem;
  {
    unsigned co = min(threadIdx.x, CODE_LINES - 1) << 7;
    u64 pc;
    asm volatile("s_getpc_b64 %0\n\ts_mov_b32 m0, %2\n\ts_nop 0\n\tglobal_load_lds_dword %1, %0" : "=&s"(pc) : "v"(co), "i"(LDS_TOTAL - 256) : "memory");
  }
  asm volatile("" :: "s"(x), "s"(wA), "s"(woA), "s"(wB), "s"(woB), "s"(gw), "s"(out), "s"(ws));
  const int lane = threadIdx.x & 63, w = __builtin_amdgcn_readfirstlane(threadIdx.x >> 6);
  const int b = blockIdx.x, xg = b & 7, r = b >> 3;
  const bool isA = (xg & 1) == 0;
  const int m = (xg >> 2) * 8 + (r & 7), p = ((xg >> 1) & 1) * 4 + (r >> 3);
  const bool act2 = r < 28;
  const int mt64 = __builtin_amdgcn_readfirstlane(xg * 4 + r / 7), nc = __builtin_amdgcn_readfirstlane(r % 7);
  const auto rs = __builtin_amdgcn_make_buffer_rsrc(ws, 0, WS_BYTES, 0x00020000);
  const int g = lane >> 4, l15 = lane & 15;
  const f32x4 zero = {0.f, 0.f, 0.f, 0.f};
  u64 tag;

  if (w >= 4) {
    const int lw = w - 4;
    const unsigned x0 = (m * 128 + lw * 8 + (lane >> 3)) * 896 + (((lane & 7) ^ g ^ ((lw & 1) << 2)) << 4);
    int wunit = (lane & 7) ^ (((g & 1) | ((lw & 1) << 1)) << 1); wunit = wunit > 6 ? 6 : wunit;
    const unsigned w0 = (lw * 8 + (lane >> 3)) * 2688 + p * 112 + wunit * 16;
    const unsigned woo = (lw * 8 + (lane >> 3)) * 896 + nc * 128 + (((lane & 7) ^ ((g & 1) << 2)) << 4);
    auto wo_b = [&]() {
      glds_wo<12>(woB, woo, lds0 + LDS_WO + 56 * 1024 + lw * 1024);
      glds16(woB + 48 * 7168, woo, lw == 0 ? lds0 + LDS_WO + 104 * 1024 : lds0 + LDS_WO2 + (lw - 1) * 1024);
      glds16(woB + 52 * 7168, woo, lds0 + LDS_WO2 + (lw + 3) * 1024);
    };
    if (isA) {
      int r0 = m * 128 - 16; r0 = r0 < 0 ? 0 : r0;
      const int hr = lw * 4 + ((lane >> 3) & 3);
      const unsigned ho = (r0 + hr) * 896 + (((lane & 7) ^ (hr >> 1)) << 4);
      int sl = 0;
#pragma unroll 1
      for (int i = 0; i < 7; ++i) {
        glds_chunk(x + i * 128, x0, x0 + 32 * 896, x0 + 64 * 896, x0 + 96 * 896, wA + i * (64 * 2688), w0, w0 + 32 * 2688, lds0 + LDS_SLOT + sl + lw * 1024);
        if (lane < 32) glds16(x + i * 128, ho, lds0 + LDS_HALO + i * 2048 + lw * 512);
        sl = sl == 2 * SLOT_SZ ? 0 : sl + SLOT_SZ;
        if (i >= 1) WAIT_BAR(11);
      }
      WAIT_BAR(0);
      if (act2) glds_wo<10>(woA, woo, lds0 + LDS_WO + lw * 1024);
      RBAR();
      if (threadIdx.x == 256) *(LAS unsigned*)(smem + LDS_CNT) = 0u;
      if (act2) glds_wo<4>(woA + 40 * 7168, woo, lds0 + LDS_WO + 40 * 1024 + lw * 1024);
      tag = read_tag(ws);
      BAR();
      if (act2) { __builtin_amdgcn_s_sleep(8); wo_b(); }
    } else {
      int sl = 0;
      bf16x8 gf[14];
#pragma unroll 1
      for (int i = 0; i < 7; ++i) {
        glds_chunk_b(x + i * 128, x0, x0 + 32 * 896, x0 + 64 * 896, x0 + 96 * 896, wB + 896 + i * (64 * 2688), w0, w0 + 32 * 2688,
                     lds0 + LDS_XR + i * 16384 + lw * 1024, lds0 + sl + lw * 1024);
        sl = sl == 32768 ? 0 : sl + 16384;
        if (i >= 1) WAIT_BAR(8);
      }
      glds_wq<4>(wB, w0, lds0 + 16384 + lw * 1024);
      WAIT_BAR(4);
      glds_wq<4>(wB + 2 * (64 * 2688), w0, lds0 + 32768 + lw * 1024);
      u32x4 wr[14], wra[14];
      const auto ra_ = __builtin_amdgcn_make_buffer_rsrc((void*)woA, 0, 448 * 896, 0x00020000), rb_ = __builtin_amdgcn_make_buffer_rsrc((void*)woB, 0, 448 * 896, 0x00020000);
      RBAR();
      if (lw == 3) {
#pragma unroll
        for (int ks = 0; ks < 14; ++ks) gf[ks] = *(const GAS bf16x8*)(gw + ks * 64 + g * 16);
      }
      MEMF();
#pragma unroll
      for (int j = 0; j < 14; ++j) wr[j] = __builtin_amdgcn_raw_buffer_load_b128(rb_, woo, j * 28672, 0);
      tag = read_tag(ws);
      WAIT_BAR(14);
      const int kq_ = (p * 4 + lw) * 16;
      {
        lptr rb = smem + lw * 1024 + lane * 16;
        const f32x4 s0 = *(const LAS f32x4*)(rb) + *(const LAS f32x4*)(rb + 8192);
        const f32x4 s1 = *(const LAS f32x4*)(rb + 4096) + *(const LAS f32x4*)(rb + 12288);
        ST16(asu(cat(pk4(s0), pk4(s1))), WS_KVP + (kq_ + m) * 1024 + lane * 16);
      }
      LGKM0();
      glds_wq<4>(wB + 4 * (64 * 2688), w0, lds0 + lw * 1024);
      RBAR();
      glds_wq<2>(wB + 6 * (64 * 2688), w0, lds0 + 16384 + lw * 1024);
      WAITV(6);
      if (lane == 0) ast(ws + WS_FLAGK + (kq_ + m) * 64, tag);
      float gv = 0.f;
      if (lw == 3) {
        lptr xa = smem + LDS_XR + (p * 16 + l15) * 128;
        const int sx = (g ^ (l15 >> 1)) << 4;
        f32x4 ga = zero, gb = zero;
        bf16x8 xr[14];
        lptr xa0 = mklp((unsigned)(size_t)(xa + sx)), xa1 = mklp((unsigned)(size_t)(xa + (sx ^ 64))), xa2 = mklp((unsigned)(size_t)(xa + sx + 65536)), xa3 = mklp((unsigned)(size_t)(xa + (sx ^ 64) + 65536));
#pragma unroll
        for (int ks = 0; ks < 14; ++ks) xr[ks] = lds_b128(ks < 8 ? ((ks & 1) ? xa1 : xa0) : ((ks & 1) ? xa3 : xa2), ((ks >> 1) & 3) * 16384);
        SBAR();
#pragma unroll
        for (int ks = 0; ks < 14; ks += 2) { ga = MFMA(gf[ks], xr[ks], ga); gb = MFMA(gf[ks + 1], xr[ks + 1], gb); }
        gv = rbf(__builtin_amdgcn_rcpf(1.f + __builtin_amdgcn_exp2f(-1.4426950408889634f * rbf(ga[0] + gb[0]))));
      }
      __builtin_amdgcn_raw_buffer_store_b32(__float_as_uint(gv), rs, (lw == 3 && lane < 16) ? WS_GATE + (m * 128 + p * 16 + lane) * 4 : WS_BYTES, 0, 16);
      MEMF();
#pragma unroll
      for (int j = 0; j < 14; ++j) wra[j] = __builtin_amdgcn_raw_buffer_load_b128(ra_, woo, j * 28672, 0);
      LGKM0();
      WAIT_BAR(14);
      if (threadIdx.x == 256) *(LAS unsigned*)(smem + LDS_CNT) = 0u;
      {
        const char* kf = ws + WS_FLAGK + (kq_ + l15) * 64;
        unsigned sp = 0;
#pragma unroll 1
        for (;;) {
          if (__all(ald(kf) == tag) || ++sp >= SPIN_CAP) break;
          __builtin_amdgcn_s_sleep(2);
        }
        MEMF();
        u32x4 k0[16];
#pragma unroll
        for (int j = 0; j < 16; ++j) k0[j] = LD16(WS_KVP + (kq_ + j) * 1024 + lane * 16);
        f32x8 s8 = __builtin_convertvector(asbf(k0[0]), f32x8);
#pragma unroll
        for (int j = 1; j < 16; ++j) s8 += __builtin_convertvector(asbf(k0[j]), f32x8);
        *(LAS bf16x8*)(smem + LDS_KVF + lw * 1024 + lane * 16) = __builtin_convertvector(s8, bf16x8);
      }
      WAITV(0);
      BAR();
#pragma unroll
      for (int j = 0; j < 14; ++j) *(LAS u32x4*)(smem + LDS_WO + lw * 1024 + j * 4096 + lane * 16) = wra[j];
#pragma unroll
      for (int j = 0; j < 12; ++j) *(LAS u32x4*)(smem + LDS_WO + 56 * 1024 + lw * 1024 + j * 4096 + lane * 16) = wr[j];
      *(LAS u32x4*)(smem + (lw == 0 ? LDS_WO + 104 * 1024 : LDS_WO2 + (lw - 1) * 1024) + lane * 16) = wr[12];
      *(LAS u32x4*)(smem + LDS_WO2 + (lw + 3) * 1024 + lane * 16) = wr[13];
    }
    if (!act2) {
      if (b == 248 && lw == 0) {
        unsigned sp = 0;
#pragma unroll 1
        for (;;) {
          bool ok = true;
#pragma unroll
          for (int j = 0; j < 4; ++j) ok &= ald(ws + WS_FLAGB + (j * 64 + lane) * 64) == tag;
          if (__all(ok) || ++sp >= SPIN_CAP) break;
          __builtin_amdgcn_s_sleep(8);
        }
        if (lane == 0) ast(ws + WS_EPOCH, tag);
      }
      return;
    }
    WAITV(0);
  } else {
    const int mh = w & 1, hw = w >> 1;
    const int h = 2 * p + hw;
    if (w < 3) {
      const char* src = w == 0 ? x + (b * 56 + min(lane, 55)) * 128 : (w == 1 ? wA : wB) + min(b * 37 + min(lane, 36), 9407) * 128;
      asm volatile("s_mov_b32 m0, %1\n\ts_nop 0\n\tglobal_load_lds_dword %0, off" :: "v"(src), "i"(LDS_TOTAL - 256) : "memory");
    }
    tag = read_tag(ws);
    P1 st;
    const bf16x4 z4 = {(__bf16)0.f, (__bf16)0.f, (__bf16)0.f, (__bf16)0.f};
    int sto = (isA ? WS_OA : WS_OB) + (((m * 4 + mh * 2) * 16 + h) * 2 + (g >> 1)) * 1024 + ((g & 1) * 32 + l15) * 16;
    PIN(sto);
    if (isA) {
      if (mh) k_pass<0, 1>(st, smem, lane, w); else k_pass<0, 0>(st, smem, lane, w);
      bf16x8 qf[4], kf[4]; bf16x4 vl[4][2];
      kf[3] = cat(pk4(st.acc[3][2]), pk4(st.acc[3][3]));
      vl[3][0] = pk4(st.acc[3][4]); vl[3][1] = pk4(st.acc[3][5]);
      lptr xs = smem + LDS_AX + hw * 4096;
      if (mh == 0) {
        *(LAS bf16x8*)(xs + lane * 16) = kf[3];
        *(LAS bf16x8*)(xs + 1024 + lane * 16) = cat(vl[3][0], vl[3][1]);
      } else {
        *(LAS bf16x4*)(xs + 2048 + l15 * 32 + g * 8) = pk4(st.accx[0]);
        *(LAS bf16x4*)(xs + 2560 + l15 * 32 + g * 8) = pk4(st.accx[1]);
      }
      BAR();
      bf16x8 kprev; bf16x4 vprev[2];
      if (mh) {
        kprev = lds_b128(xs, lane * 16);
        bf16x8 t = lds_b128(xs, 1024 + lane * 16);
        vprev[0] = __builtin_shufflevector(t, t, 0, 1, 2, 3); vprev[1] = __builtin_shufflevector(t, t, 4, 5, 6, 7);
      } else {
        kprev = cat(pk4(st.accx[0]), pk4(st.accx[1]));
        vprev[0] = lds_tr(xs, 2048 + lane * 8);
        vprev[1] = lds_tr(xs, 2560 + lane * 8);
      }
      f32x4 cprev, ccur, cprev0;
#pragma unroll
      for (int r = 0; r < 4; ++r) {
        const int j = 4 * g + r; const bool par = ((l15 ^ r) & 1) == 0;
        cprev[r] = (par && j >= l15 + 2) ? 0.f : -3.0e38f;
        ccur[r] = (par && j <= l15) ? 0.f : -3.0e38f;
        cprev0[r] = (mh == 0 && m == 0) ? -3.0e38f : cprev[r];
      }
#pragma unroll
      for (int i = 0; i < 4; ++i) {
        bf16x4 hi = pk4(st.acc[i][1]); hi = g == 3 ? z4 : hi;
        qf[i] = cat(pk4(st.acc[i][0]), hi);
        if (i < 3) { kf[i] = cat(pk4(st.acc[i][2]), pk4(st.acc[i][3])); vl[i][0] = pk4(st.acc[i][4]); vl[i][1] = pk4(st.acc[i][5]); }
      }
      const float c2 = 0.1889822365046136f * 1.4426950408889634f;
      const unsigned hmask = (lane & 1) ? 0xffff0000u : 0x0000ffffu;
      f32x4 sp[4], sc[4], e[4]; float mx[4], rsum[4]; bf16x8 pf[4];
#pragma unroll
      for (int i = 3; i >= 0; --i) { sc[i] = MFMA(kf[i], qf[i], ccur); sp[i] = i ? MFMA(kf[i ? i - 1 : 0], qf[i], cprev) : MFMA(kprev, qf[0], cprev0); }
#pragma unroll
      for (int i = 3; i >= 0; --i) {
        e[i][0] = MAXF(sp[i][0], sp[i][1]); e[i][1] = MAXF(sp[i][2], sp[i][3]); e[i][2] = MAXF(sc[i][0], sc[i][1]); e[i][3] = MAXF(sc[i][2], sc[i][3]);
        mx[i] = MAXF(MAXF(e[i][0], e[i][1]), MAXF(e[i][2], e[i][3]));
      }
#pragma unroll
      for (int i = 3; i >= 0; --i) mx[i] = xl16(mx[i], true);
#pragma unroll
      for (int i = 3; i >= 0; --i) mx[i] = xl32(mx[i], true) * c2;
      f32x4 o0[4], o1[4];
#pragma unroll
      for (int i = 3; i >= 0; --i) {
        u32x4 pu;
#pragma unroll
        for (int r = 0; r < 4; ++r) {
          e[i][r] = __builtin_amdgcn_exp2f(__builtin_fmaf(e[i][r], c2, -mx[i]));
          f32x2 t = {e[i][r], e[i][r]}; pu[r] = __builtin_bit_cast(unsigned, __builtin_convertvector(t, bf16x2)) & hmask;
        }
        pf[i] = asbf(pu);
        rsum[i] = (e[i][0] + e[i][1]) + (e[i][2] + e[i][3]);
        o0[i] = MFMA(cat(i == 0 ? vprev[0] : vl[i ? i - 1 : 0][0], vl[i][0]), pf[i], zero);
        o1[i] = MFMA(cat(i == 0 ? vprev[1] : vl[i ? i - 1 : 0][1], vl[i][1]), pf[i], zero);
      }
#pragma unroll
      for (int i = 3; i >= 0; --i) rsum[i] = xl16(rsum[i], false);
#pragma unroll
      for (int i = 3; i >= 0; --i) rsum[i] = __builtin_amdgcn_rcpf(xl32(rsum[i], false));
#pragma unroll
      for (int i = 3; i >= 0; --i) {
        const float r1 = g == 3 ? 0.f : rsum[i];
        ST16(asu(cat(pk4(o0[i] * rsum[i]), pk4(o1[i] * r1))), sto + (i >> 1) * 32768 + (i & 1) * 256);
      }
    } else {
      int lp1 = 1024 + ((lane & 48) | (l15 < 12 ? (l15 / 3) * 4 + l15 % 3 : (l15 - 12) * 4 + 3)) * 16;
      PIN(lp1);
      k_pass<1, 0>(st, smem, lane, w);
      const bf16x8 ones = {(__bf16)1.f, (__bf16)1.f, (__bf16)1.f, (__bf16)1.f, (__bf16)1.f, (__bf16)1.f, (__bf16)1.f, (__bf16)1.f};
      f32x4 kv[2][2];
#pragma unroll
      for (int u = 0; u < 2; ++u) {
        bf16x8 pkf[2], vf[2];
#pragma unroll
        for (int t = 0; t < 2; ++t) {
          pkf[t] = cat(pk4(phi4(st.acc[2 * u][t])), pk4(phi4(st.acc[2 * u + 1][t])));
          vf[t] = cat(pk4(st.acc[2 * u][2 + t]), pk4(st.acc[2 * u + 1][2 + t]));
        }
        vf[1] = l15 >= 12 ? ones : vf[1];
#pragma unroll
        for (int td = 0; td < 2; ++td)
#pragma unroll
          for (int te = 0; te < 2; ++te) kv[td][te] = MFMA(pkf[td], vf[te], u ? kv[td][te] : zero);
      }
      lptr rw = smem + hw * 2048 + mh * 8192;
#pragma unroll
      for (int td = 0; td < 2; ++td) {
        *(LAS f32x4*)(rw + td * 4096 + lane * 16) = kv[td][0];
        *(LAS f32x4*)(rw + td * 4096 + lp1) = kv[td][1];
      }
      BAR();
      SBAR();
      q_pass(st, smem, lane, w);
      bf16x8 pq[4];
#pragma unroll
      for (int i = 0; i < 4; ++i) {
        bf16x4 hi = pk4(phi4(st.acc[i][1])); hi = g == 3 ? z4 : hi;
        pq[i] = cat(pk4(phi4(st.acc[i][0])), hi);
        PIN(pq[i]);
      }
      RBAR();
      const bf16x8 kv0 = lds_b128(smem, LDS_KVF + hw * 2048 + lane * 16), kv1 = lds_b128(smem, LDS_KVF + hw * 2048 + 1024 + lane * 16);
      f32x4 y0[4], y1[4];
#pragma unroll
      for (int i = 0; i < 4; ++i) { y1[i] = MFMA(kv1, pq[i], zero); y0[i] = MFMA(kv0, pq[i], zero); }
#pragma unroll
      for (int i = 0; i < 4; ++i) {
        const float rd = __builtin_amdgcn_rcpf(y1[i][3] + 1e-6f);
        f32x4 c = y1[i] * rd; c[3] = 0.f;
        ST16(asu(cat(pk4(y0[i] * rd), pk4(c))), sto + (i >> 1) * 32768 + (i & 1) * 256);
      }
    }
    WAITV(0);
    if (lane == 0) {
      const unsigned old = __hip_atomic_fetch_add((LAS unsigned*)(smem + LDS_CNT), 1u, __ATOMIC_RELAXED, __HIP_MEMORY_SCOPE_WORKGROUP);
      if (old == 3) ast(ws + (isA ? WS_FLAGA : WS_FLAGB) + (m * 8 + p) * 64, tag);
    }
    if (!act2) return;
  }
  const int th = w & 1, kq = w >> 1;
  const int h2 = lane >> 5, blk = (lane >> 4) & 1, q = l15 >> 2, pp = lane & 3;
  lptr a0[2], b0[2], b1[2][2];
#pragma unroll
  for (int u = 0; u < 2; ++u) {
    const int colb = (u ^ (kq >> 1)) * 64 + (blk ^ (kq & 1)) * 32 + (((pp & 1) << 1) | (pp >> 1)) * 8;
    const int o0 = 4 * h2 + q;
    const unsigned ua = lds0 + LDS_WO + kq * 14336 + o0 * 128 + (colb ^ (((o0 >> 1) & 1) << 6));
    a0[u] = mklp(ua); b0[u] = mklp(ua + 448 * 128);
#pragma unroll
    for (int s = 0; s < 2; ++s) {
      const int o1 = 16 + 6 * s + 3 * h2 + (q > 2 ? 2 : q);
      b1[s][u] = mklp(lds0 + LDS_WO + 448 * 128 + kq * 14336 + o1 * 128 + (colb ^ (((o1 >> 1) & 1) << 6)));
    }
  }
  int abase = ((mt64 * 2 + th) * 16 + kq * 4) * 2048 + lane * 16; PIN(abase);
  unsigned orow = (mt64 * 64 + th * 32 + (lane & 31)) * 896 + nc * 128 + kq * 32 + h2 * 16; PIN(orow);
  const char* fp = ws + ((lane & 6) ? WS_FLAGB : WS_FLAGA) + ((mt64 >> 1) * 8 + ((lane & 4) ? (mt64 & 1) * 4 + th * 2 : kq * 2) + (lane & 1)) * 64;
  BAR();
  bf16x8 wf[8][2];
#pragma unroll
  for (int j = 0; j < 8; ++j)
#pragma unroll
    for (int u = 0; u < 2; ++u) wf[j][u] = cat(lds_tr(a0[u], (j >> 1) * 3584 + (j & 1) * 1024), lds_tr(a0[u], (j >> 1) * 3584 + (j & 1) * 1024 + 2048));
  BAR();
  u64 rdy = 0; unsigned sp = 0;
  auto wait_flags = [&](u64 need) {
#pragma unroll 1
    while ((rdy & need) != need && ++sp < SPIN_CAP) { rdy = __ballot(ald(fp) == tag); if ((rdy & need) != need) __builtin_amdgcn_s_sleep(2); }
    MEMF();
  };
  wait_flags(3);
  u32x4 af[8], bf[8];
#pragma unroll
  for (int j = 0; j < 8; ++j) { af[j] = LD16(WS_OA + abase + j * 1024); SBAR(); }
  const bool early = (rdy & 0x3c) == 0x3c;
  int bo = early ? WS_OB + abase : WS_BYTES, go = early ? WS_GATE + (mt64 * 64 + th * 32 + (lane & 31)) * 4 : WS_BYTES;
#pragma unroll
  for (int j = 0; j < 8; ++j) { bf[j] = LD16(bo + j * 1024); SBAR(); }
  unsigned gu = __builtin_amdgcn_raw_buffer_load_b32(rs, go, 0, 16);
  SBAR();
  f32x16 acc[2], acb[2];
  const int w2 = kq == 3 ? LDS_WO2 - LDS_WO - 840 * 128 : 0;
#pragma unroll
  for (int j = 0; j < 8; ++j) {
#pragma unroll
    for (int u = 0; u < 2; ++u) acc[u] = MFMA32(wf[j][u], asbf(af[j]), j ? acc[u] : f32x16{});
    SBAR();
    if (j == 4) { b0[0] += w2; b0[1] += w2; b1[0][0] += w2; b1[0][1] += w2; b1[1][0] += w2; b1[1][1] += w2; }
#pragma unroll
    for (int u = 0; u < 2; ++u) wf[j][u] = cat(lds_tr(b0[u], (j >> 1) * 3584 + (j & 1) * 1024), lds_tr(b1[j & 1][u], (j >> 1) * 3584));
    SBAR();
  }
  if (!early) {
    wait_flags(0x3c);
#pragma unroll
    for (int j = 0; j < 8; ++j) { bf[j] = LD16(WS_OB + abase + j * 1024); SBAR(); }
    gu = __builtin_amdgcn_raw_buffer_load_b32(rs, WS_GATE + (mt64 * 64 + th * 32 + (lane & 31)) * 4, 0, 16);
  }
  SBAR();
#pragma unroll
  for (int j = 0; j < 8; ++j) {
#pragma unroll
    for (int u = 0; u < 2; ++u) acb[u] = MFMA32(wf[j][u], asbf(bf[j]), j ? acb[u] : f32x16{});
    SBAR();
  }
  const float gv = __uint_as_float(gu), og = rbf(1.f - gv);
  acc[1] = acc[1] * gv + acb[1] * og;
  acc[0] = acc[0] * gv + acb[0] * og;
  {
    lptr xw = smem + LDS_WO + kq * 14336 + 1024 + th * 6144 + lane * 16;
#pragma unroll
    for (int c = 1; c < 4; ++c) {
      const f32x16 a = acc[c >> 1];
      const int o = (c & 1) * 8;
      *(LAS f32x4*)(xw + (c - 1) * 2048) = f32x4{a[o], a[o + 1], a[o + 2], a[o + 3]};
      *(LAS f32x4*)(xw + (c - 1) * 2048 + 1024) = f32x4{a[o + 4], a[o + 5], a[o + 6], a[o + 7]};
    }
    BAR();
    f32x4 lo = {acc[0][0], acc[0][1], acc[0][2], acc[0][3]}, hi = {acc[0][4], acc[0][5], acc[0][6], acc[0][7]};
    f32x4 rl[3], rh[3];
#pragma unroll
    for (int c = 1; c < 4; ++c) {
      lptr xr = smem + LDS_WO + (kq ^ c) * 14336 + 1024 + th * 6144 + (c - 1) * 2048 + lane * 16;
      rl[c - 1] = *(const LAS f32x4*)(xr); rh[c - 1] = *(const LAS f32x4*)(xr + 1024);
    }
    lo = (lo + rl[0]) + (rl[1] + rl[2]); hi = (hi + rh[0]) + (rh[1] + rh[2]);
    *(bf16x8*)(out + orow) = cat(pk4(lo), pk4(hi));
  }
}

extern "C" void kernel_launch(void* const* d_in, const int* in_sizes, int n_in, void* d_out, int out_size, void* d_ws, size_t ws_size,
                              hipStream_t stream) {
  (void)hipFuncSetAttribute((const void*)gated_mix__sliding_local_dilated__linear_attn_block__prompt_kernel, hipFuncAttributeMaxDynamicSharedMemorySize, LDS_TOTAL);
  gated_mix__sliding_local_dilated__linear_attn_block__prompt_kernel<<<256, 512, LDS_TOTAL, stream>>>((const char*)d_in[0], (const char*)d_in[1], (const char*)d_in[2], (const char*)d_in[4],
                                                (const char*)d_in[5], (const char*)d_in[6], (char*)d_out, (char*)d_ws);
}
```
